```python
import torch

def _softmax(x, axis=-1):
    return torch.softmax(x, dim=axis)
def _gelu(x):
    return torch.nn.functional.gelu(x, approximate="tanh")
def _silu(x):
    return torch.nn.functional.silu(x)
def _sigmoid(x):
    return torch.sigmoid(x)
def _layernorm(x, axis=-1, eps=1e-5):
    m = x.mean(dim=axis, keepdim=True)
    v = x.var(dim=axis, keepdim=True, unbiased=False)
    return (x - m) / torch.sqrt(v + eps)
def _scatter_add(tgt, idx, src, axis=0):
    return tgt.clone().index_add_(axis, idx, src)
def _segment_sum(src, seg, n):
    out = torch.zeros((n,) + src.shape[1:], dtype=src.dtype)
    return out.index_add_(0, seg, src)
def _pad(x, pw):
    flat = tuple(v for pair in reversed(pw) for v in pair)
    return torch.nn.functional.pad(x, flat)

kernel_name = 'concat_heads__grouped_kv_mqa__spectral_gfnet__prompt'

def setup_inputs():

    _w = (lambda a: (lambda t: t.to(torch.bfloat16) if t.is_floating_point() else t)(torch.as_tensor(a)))
    def _A_setup():
        B, S, D, H = 1, 2048, 576, 32
        Dh = D // H
        return {
            "x": _w((lambda *s: torch.randn(*s, dtype=torch.bfloat16) * 0.05)(B, S, D)),
            "wq": _w((lambda *s: torch.randn(*s, dtype=torch.bfloat16) * 0.8)(D, H * Dh)),
            "wk": _w((lambda *s: torch.randn(*s, dtype=torch.bfloat16) * 0.8)(D, Dh)),
            "wv": _w((lambda *s: torch.randn(*s, dtype=torch.bfloat16) * 0.05)(D, Dh)),
            "wo": _w((lambda *s: torch.randn(*s, dtype=torch.bfloat16) * 0.05)(H * Dh, D)),
        }
    _A_in = _A_setup()

    def _B_setup():
        B, S, D = 1, 2048, 576
        Df = D // 2 + 1
        return {
            "x": _w((lambda *s: torch.randn(*s, dtype=torch.bfloat16) * 0.05)(B, S, D)),
            "w_r": _w((lambda *s: torch.randn(*s, dtype=torch.bfloat16) * 0.8)(S, Df) * 0.02),
            "w_i": _w((lambda *s: torch.randn(*s, dtype=torch.bfloat16) * 0.8)(S, Df) * 0.02),
        }
    _B_in = _B_setup()

    return {
        "A_x": _A_in["x"],
        "A_wq": _A_in["wq"],
        "A_wk": _A_in["wk"],
        "A_wv": _A_in["wv"],
        "A_wo": _A_in["wo"],
        "B_x": _B_in["x"],
        "B_w_r": _B_in["w_r"],
        "B_w_i": _B_in["w_i"],
    }

def reference(A_x, A_wq, A_wk, A_wv, A_wo, B_x, B_w_r, B_w_i):
    def _A_ref(x, wq, wk, wv, wo):
        B, S, D = x.shape
        H = 32
        Dh = D // H
        q = (lambda x, *d: x.permute(*d))((x @ wq).reshape(B, S, H, Dh), 0, 2, 1, 3)
        k = x @ wk
        v = x @ wv
        scores = torch.einsum("bhqd,bkd->bhqk", q, k) * (Dh ** -0.5)
        p = _softmax(scores, axis=-1)
        o = torch.einsum("bhqk,bkd->bhqd", p, v)
        return (lambda x, *d: x.permute(*d))(o, 0, 2, 1, 3).reshape(B, S, H * Dh) @ wo

    def _B_ref(x, w_r, w_i):
        B, S, D = x.shape
        X = (lambda x, axes=(-2,-1): torch.fft.rfft2(x.float(), dim=axes))(x, axes=(-2, -1))
        W = w_r + 1j * w_i
        X = X * W
        y = (lambda x, s, axes=(-2,-1): torch.fft.irfft2(x, s=s, dim=axes).to(torch.bfloat16))(X, (S, D), axes=(-2, -1))
        return y

    a = _A_ref(A_x, A_wq, A_wk, A_wv, A_wo)
    b = _B_ref(B_x, B_w_r, B_w_i)
    return torch.concatenate([a, b], axis=-1)
```

```cpp
#include <hip/hip_runtime.h>
#include <hip/hip_bf16.h>
#include <stdint.h>
#include <type_traits>
#include <utility>

namespace {

typedef __bf16 bf16x8 __attribute__((ext_vector_type(8)));
typedef __bf16 bf16x2 __attribute__((ext_vector_type(2)));
typedef short s16x4 __attribute__((ext_vector_type(4)));
typedef float f32x2 __attribute__((ext_vector_type(2)));
typedef float f32x4 __attribute__((ext_vector_type(4)));
typedef float f32x16 __attribute__((ext_vector_type(16)));
typedef unsigned u32x2 __attribute__((ext_vector_type(2)));
typedef unsigned u32x4 __attribute__((ext_vector_type(4)));
typedef unsigned short u16;
#define LDSP __attribute__((address_space(3)))
#define DI __device__ __forceinline__
#define SB() __builtin_amdgcn_sched_barrier(0)
#define PIN(x) asm volatile("" : "+v"(x))

constexpr int SEQ = 2048, DM = 576, DH = 18;
constexpr int TILE_B = 2432;
constexpr int KV_K1 = 1024, KV_V = 1152, KV_VH = 320;
constexpr unsigned ONES = 0x3f803f80u;
constexpr int WB = 0, SC1 = 16;
constexpr int YS = 292;
constexpr int SRD3 = 0x00020000;

constexpr size_t WS_Q = 0;
constexpr size_t WS_KV = WS_Q + (size_t)SEQ * DM * 2;
constexpr size_t WS_KMAX = WS_KV + 163840;
constexpr size_t WS_O = WS_Q;
constexpr size_t WS_Y = WS_KMAX + 1024;
constexpr size_t WS_Z = WS_Y;

constexpr double kPi = 3.14159265358979323846;
constexpr double series_sin(double x) { double t = x, s = x; for (int k = 1; k < 12; ++k) { t *= -x * x / ((2 * k) * (2 * k + 1)); s += t; } return s; }
constexpr double series_cos(double x) { double t = 1, s = 1; for (int k = 1; k < 12; ++k) { t *= -x * x / ((2 * k - 1) * (2 * k)); s += t; } return s; }
constexpr unsigned short to_bf16(double v) { const unsigned u = __builtin_bit_cast(unsigned, (float)v); return (unsigned short)((u + 0x7fffu + ((u >> 16) & 1u)) >> 16); }
template <int N> struct Trig { unsigned short c[N], s[N]; };
template <int N> constexpr Trig<N> make_trig() {
    Trig<N> t{};
    for (int m = 0; m < N; ++m) {
        double x = 2 * kPi * m / N, c = 0, s = 0;
        if (x > kPi) x -= 2 * kPi;
        if (x > kPi / 2) { c = -series_cos(kPi - x); s = series_sin(kPi - x); }
        else if (x < -kPi / 2) { c = -series_cos(-kPi - x); s = series_sin(-kPi - x); }
        else { c = series_cos(x); s = series_sin(x); }
        if (4 * m % N == 0) { const int q = 4 * m / N; c = q == 0 ? 1 : q == 2 ? -1 : 0; s = q == 1 ? 1 : q == 3 ? -1 : 0; }
        t.c[m] = to_bf16(c); t.s[m] = to_bf16(s);
    }
    return t;
}
constexpr Trig<576> kT576 = make_trig<576>();
constexpr Trig<64> kT64 = make_trig<64>();
struct Tab1 { unsigned v[10][4][64][4]; };
constexpr Tab1 make_tab1() {
    Tab1 t{};
    for (int j1 = 0; j1 < 10; ++j1) for (int k = 0; k < 4; ++k) for (int l = 0; l < 64; ++l) for (int i = 0; i < 4; ++i) {
        const int m = ((8 * k + 4 * (l >> 5) + i) * (j1 + 18 * (l & 31))) % 576;
        t.v[j1][k][l][i] = kT576.c[m] | ((unsigned)kT576.s[m] << 16);
    }
    return t;
}
struct Tab3 { unsigned v[10][2][2][64][4]; };
constexpr Tab3 make_tab3() {
    Tab3 t{};
    for (int j1 = 0; j1 < 10; ++j1) for (int u = 0; u < 2; ++u) for (int l = 0; l < 64; ++l) for (int i = 0; i < 4; ++i) {
        const int kd = j1 + 18 * (16 * u + 8 * (i >> 1) + 4 * (l >> 5) + 2 * (i & 1));
        const int m0 = ((l & 31) * kd) % 576, m1 = ((l & 31) * (kd + 18)) % 576;
        t.v[j1][u][0][l][i] = kT576.c[m0] | ((unsigned)kT576.c[m1] << 16);
        t.v[j1][u][1][l][i] = kT576.s[m0] | ((unsigned)kT576.s[m1] << 16);
    }
    return t;
}
struct Tab2 { unsigned v[4][2][2][64][4]; };
constexpr Tab2 make_tab2() {
    Tab2 t{};
    for (int m = 0; m < 4; ++m) for (int ks = 0; ks < 2; ++ks) for (int l = 0; l < 64; ++l) for (int i = 0; i < 4; ++i) {
        const int row = 16 * m + (l & 15), k = 32 * ks + 16 * (i >> 1) + 4 * (l >> 4) + 2 * (i & 1);
        const int m0 = (row * k) % 64, m1 = (row * (k + 1)) % 64;
        t.v[m][ks][0][l][i] = kT64.c[m0] | ((unsigned)kT64.c[m1] << 16);
        t.v[m][ks][1][l][i] = kT64.s[m0] | ((unsigned)kT64.s[m1] << 16);
    }
    return t;
}

__constant__ const Tab1 kTab1 = make_tab1();
__constant__ const Tab2 kTab2 = make_tab2();
__constant__ const Tab3 kTab3 = make_tab3();

constexpr float QSCALE = 0.23570226039551584f * 1.4426950408889634f;

DI unsigned cvtpk(float lo, float hi) {
    f32x2 v = {lo, hi};
    return __builtin_bit_cast(unsigned, __builtin_convertvector(v, bf16x2));
}
DI float dot2(unsigned a, unsigned b, float c) {
    return __builtin_amdgcn_fdot2_f32_bf16(__builtin_bit_cast(bf16x2, a), __builtin_bit_cast(bf16x2, b), c, false);
}
DI float bf_lo(unsigned w) { return __uint_as_float(w << 16); }
DI f32x16 mfma16(u32x4 a, u32x4 b, f32x16 c) {
    return __builtin_amdgcn_mfma_f32_32x32x16_bf16(__builtin_bit_cast(bf16x8, a), __builtin_bit_cast(bf16x8, b), c, 0, 0, 0);
}
DI f32x16 mfma8(u32x2 a, u32x2 b, f32x16 c) {
    return __builtin_amdgcn_mfma_f32_32x32x8bf16_1k(__builtin_bit_cast(s16x4, a), __builtin_bit_cast(s16x4, b), c, 0, 0, 0);
}
DI f32x4 mfmas(u32x4 a, u32x4 b, f32x4 c) {
    return __builtin_amdgcn_mfma_f32_16x16x32_bf16(__builtin_bit_cast(bf16x8, a), __builtin_bit_cast(bf16x8, b), c, 0, 0, 0);
}
DI float cosr(float r) { return __builtin_amdgcn_cosf(r); }
DI float sinr(float r) { return __builtin_amdgcn_sinf(r); }
DI unsigned rfl(unsigned x) { return __builtin_amdgcn_readfirstlane(x); }
extern __shared__ __attribute__((aligned(16))) char smem[];
DI LDSP char* lds_p(unsigned a) { return (LDSP char*)smem + a; }
DI u32x4 lds_r128(unsigned a) { return *(const LDSP u32x4*)lds_p(a); }
DI u32x2 lds_r64(unsigned a) { return *(const LDSP u32x2*)lds_p(a); }
DI unsigned lds_r32(unsigned a) { return *(const LDSP unsigned*)lds_p(a); }
DI unsigned lds_poll(unsigned a) { return rfl(*(const volatile LDSP unsigned*)lds_p(a)); }
DI void lds_post(unsigned a, unsigned v) { *(volatile LDSP unsigned*)lds_p(a) = v; }
DI void lds_w32(unsigned a, unsigned v) { *(LDSP unsigned*)lds_p(a) = v; }
DI u32x2 lds_tr(unsigned a) {
    return __builtin_bit_cast(u32x2, __builtin_amdgcn_ds_read_tr16_b64_v4i16((LDSP s16x4*)lds_p(a)));
}
DI unsigned lds_abs(unsigned a) { return rfl((unsigned)(size_t)lds_p(a)); }
DI void glds16(const void* g, unsigned lds_off) {
    const unsigned lds_addr = lds_abs(lds_off);
    unsigned keep;
    asm volatile("s_mov_b32 %0, m0\n\ts_mov_b32 m0, %2\n\ts_nop 0\n\tglobal_load_lds_dwordx4 %1, off\n\ts_mov_b32 m0, %0"
                 : "=&s"(keep) : "v"(g), "s"(lds_addr) : "memory");
}
DI void glds4(const void* g, unsigned lds_off) {
    const unsigned lds_addr = lds_abs(lds_off);
    unsigned keep;
    asm volatile("s_mov_b32 %0, m0\n\ts_mov_b32 m0, %2\n\ts_nop 0\n\tglobal_load_lds_dword %1, off\n\ts_mov_b32 m0, %0"
                 : "=&s"(keep) : "v"(g), "s"(lds_addr) : "memory");
}
#define CODE_OF(name, sym) DI const char* name() { \
    unsigned lo, hi; \
    asm volatile("s_getpc_b64 s[40:41]\n\ts_add_u32 s40, s40, " sym "@rel32@lo+4\n\ts_addc_u32 s41, s41, " sym "@rel32@hi+12\n\t" \
                 "s_mov_b32 %0, s40\n\ts_mov_b32 %1, s41" : "=s"(lo), "=s"(hi) :: "s40", "s41", "scc"); \
    return (const char*)(((unsigned long long)hi << 32) | lo); }
CODE_OF(code_of_k2, "_Z12k2_attn_fft2PKtS0_S0_Pc")
CODE_OF(code_of_k3, "_Z12k3_proj_fft3PKtPcPt")
DI void glds12(const void* g, unsigned lds_off) {
    const unsigned lds_addr = lds_abs(lds_off);
    unsigned keep;
    asm volatile("s_mov_b32 %0, m0\n\ts_mov_b32 m0, %2\n\ts_nop 0\n\tglobal_load_lds_dwordx3 %1, off\n\ts_mov_b32 m0, %0"
                 : "=&s"(keep) : "v"(g), "s"(lds_addr) : "memory");
}
template <int N> DI void wait_vm() { asm volatile("s_waitcnt vmcnt(%0)" :: "i"(N) : "memory"); }
DI void bar() { asm volatile("s_waitcnt lgkmcnt(0)\n\ts_barrier" ::: "memory"); }
DI float other_half(float x) {
    auto r = __builtin_amdgcn_permlane32_swap(__float_as_uint(x), __float_as_uint(x), false, false);
    return (threadIdx.x & 32) ? __uint_as_float(r[0]) : __uint_as_float(r[1]);
}
DI float wave_max(float x) {
    x = fmaxf(x, __builtin_bit_cast(float, __builtin_amdgcn_update_dpp(0, __builtin_bit_cast(int, x), 0xB1, 0xf, 0xf, true)));
    x = fmaxf(x, __builtin_bit_cast(float, __builtin_amdgcn_update_dpp(0, __builtin_bit_cast(int, x), 0x4E, 0xf, 0xf, true)));
    x = fmaxf(x, __builtin_bit_cast(float, __builtin_amdgcn_update_dpp(0, __builtin_bit_cast(int, x), 0x141, 0xf, 0xf, true)));
    x = fmaxf(x, __builtin_bit_cast(float, __builtin_amdgcn_update_dpp(0, __builtin_bit_cast(int, x), 0x140, 0xf, 0xf, true)));
    { auto r = __builtin_amdgcn_permlane16_swap(__float_as_uint(x), __float_as_uint(x), false, false);
      x = fmaxf(__uint_as_float(r[0]), __uint_as_float(r[1])); }
    { auto r = __builtin_amdgcn_permlane32_swap(__float_as_uint(x), __float_as_uint(x), false, false);
      x = fmaxf(__uint_as_float(r[0]), __uint_as_float(r[1])); }
    return x;
}
DI int crow(int reg, int h) { return (reg & 3) + 8 * (reg >> 2) + 4 * h; }
DI u32x4 pack8(const f32x16& a, int u) {
    u32x4 r;
    r[0] = cvtpk(a[8 * u + 0], a[8 * u + 1]); r[1] = cvtpk(a[8 * u + 2], a[8 * u + 3]);
    r[2] = cvtpk(a[8 * u + 4], a[8 * u + 5]); r[3] = cvtpk(a[8 * u + 6], a[8 * u + 7]);
    return r;
}
DI u32x4 xor4(u32x4 a, unsigned m) { return u32x4{a[0] ^ m, a[1] ^ m, a[2] ^ m, a[3] ^ m}; }
DI __amdgpu_buffer_rsrc_t make_srd(const void* p, unsigned bytes) {
    return __builtin_amdgcn_make_buffer_rsrc((void*)p, (short)0, (int)bytes, SRD3);
}

template <int AUX = SC1> DI void st32(__amdgpu_buffer_rsrc_t r, unsigned off, unsigned v) { __builtin_amdgcn_raw_buffer_store_b32(v, r, off, 0, AUX); }
template <int AUX = SC1> DI void st128(__amdgpu_buffer_rsrc_t r, unsigned off, u32x4 v) { __builtin_amdgcn_raw_buffer_store_b128(v, r, off, 0, AUX); }

constexpr int G_X = 0, G_W = 8192, G_KV = 20480, G_BUF = 23552, G_NBUF = 6;
constexpr int G_STAGE = 3 * G_BUF, G_SROW = 208;

struct GemmFrag { u32x2 w0, w1; u32x4 x0, x1; };
template <int WSTRIDE, int I>
DI void gemm_read(GemmFrag& f, unsigned xa, unsigned xs0, unsigned xs1, int h, unsigned wa) {
    constexpr unsigned b = ((I >> 2) % G_NBUF) * G_BUF;
    constexpr int ks = I & 3;
    f.w0 = lds_tr(b + wa + (16 * ks) * WSTRIDE);
    f.w1 = lds_tr(b + wa + (16 * ks + 4) * WSTRIDE);
    f.x0 = lds_r128(b + xa + (((2 * ks + h) ^ xs0) << 4));
    f.x1 = lds_r128(b + xa + 4096 + (((2 * ks + h) ^ xs1) << 4));
}
template <int WSTRIDE, bool SWAP, int I>
DI void gemm_kstep(GemmFrag (&f)[3], f32x16 (&acc)[2], unsigned xa, unsigned xs0, unsigned xs1, int h, unsigned wa) {
    if constexpr ((I & 3) == 0 && (I >> 2) <= 7) asm volatile("s_barrier" ::: "memory");
    if constexpr (I == 0) { gemm_read<WSTRIDE, 0>(f[0], xa, xs0, xs1, h, wa); gemm_read<WSTRIDE, 1>(f[1], xa, xs0, xs1, h, wa); }
    if constexpr (I + 2 < 36) gemm_read<WSTRIDE, I + 2>(f[(I + 2) % 3], xa, xs0, xs1, h, wa);
    SB();
    const GemmFrag& c = f[I % 3];
    const u32x4 wf = {c.w0[0], c.w0[1], c.w1[0], c.w1[1]};
    acc[0] = SWAP ? mfma16(c.x0, wf, acc[0]) : mfma16(wf, c.x0, acc[0]);
    acc[1] = SWAP ? mfma16(c.x1, wf, acc[1]) : mfma16(wf, c.x1, acc[1]);
    SB();
}
template <int WSTRIDE, bool SWAP, int... Is>
DI void gemm_loop(f32x16 (&acc)[2], unsigned xa, unsigned xs0, unsigned xs1, int h, unsigned wa, std::integer_sequence<int, Is...>) {
    GemmFrag f[3];
    (gemm_kstep<WSTRIDE, SWAP, Is>(f, acc, xa, xs0, xs1, h, wa), ...);
}

template <bool QKV>
DI void gemm_role(int rb, int cb, const u16* __restrict__ X, const u16* __restrict__ W, const u16* __restrict__ Wkv,
                  u16* __restrict__ Out, int out_stride, char* __restrict__ kvimg) {
    const int tid = threadIdx.x, lane = tid & 63, l31 = lane & 31, h = lane >> 5;
    const int w = rfl(tid >> 6);
    const int t0 = rb * 64, n0 = cb * 96;
    const bool kv = QKV && cb < 2;

    if (w >= 4) {
        const int l = w - 4;
        auto wsrc = [&](int p) { const int idx = p * 64 + lane, row = idx / 12, c = idx % 12;
                                 return (const char*)W + (size_t)row * (DM * 2) + n0 * 2 + c * 16; };
        const char* s0;
        { const int row = 8 * l + (lane >> 3), pos = lane & 7;
          s0 = (const char*)X + (size_t)(t0 + row) * (DM * 2) + ((pos ^ ((row >> 1) & 7)) << 4); }
        const char* s1 = wsrc(l);
        const char* s2;
        if (l < 4) s2 = wsrc(l + 8);
        else { const int idx = (l < 7 ? l - 4 : 0) * 64 + lane; s2 = (const char*)Wkv + (idx / 3) * 36 + (idx % 3) * 12; }
        const bool three = l < 4 || (kv && l < 7);
        auto issue = [&](int c) {
            const unsigned b = (unsigned)((c % G_NBUF) * G_BUF);
            glds16(s0 + c * 128, b + G_X + l * 1024);
            glds16(s1 + (size_t)c * (64 * DM * 2), b + G_W + l * 1024);
            if (l < 4) glds16(s2 + (size_t)c * (64 * DM * 2), b + G_W + (l + 8) * 1024);
            else if (three) glds12(s2 + c * (64 * 36), b + G_KV + (l - 4) * 1024);
        };
        issue(0); issue(1); issue(2); issue(3);
#pragma unroll
        for (int c = 0; c < 8; ++c) {
            if (c <= 5) { if (three) wait_vm<6>(); else wait_vm<4>(); }
            else if (c == 6) { if (three) wait_vm<3>(); else wait_vm<2>(); }
            else wait_vm<0>();
            asm volatile("s_barrier" ::: "memory");
            if (c + 4 <= 8) issue(c + 4);
        }
        return;
    }
    f32x16 acc[2] = {};
    const unsigned xa = G_X + l31 * 128, xs0 = (l31 >> 1) & 7, xs1 = ((l31 + 32) >> 1) & 7;
    const int blk = (lane >> 4) & 1, q = (lane & 15) >> 2, p = lane & 3;
    if (w < 3) {
        const unsigned wa = G_W + (8 * h + q) * 192 + (32 * w + 16 * blk + 4 * p) * 2;
        gemm_loop<192, false>(acc, xa, xs0, xs1, h, wa, std::make_integer_sequence<int, 36>{});
        const float sc = QKV ? QSCALE : 1.0f;
#pragma unroll
        for (int tt = 0; tt < 2; ++tt) {
            const unsigned o = G_STAGE + (32 * tt + l31) * G_SROW + (32 * w + 8 * h) * 2;
#pragma unroll
            for (int g = 0; g < 4; g += 2) {
                const f32x16& a = acc[tt];
                const auto x = __builtin_amdgcn_permlane32_swap(cvtpk(a[4 * g] * sc, a[4 * g + 1] * sc), cvtpk(a[4 * g + 4] * sc, a[4 * g + 5] * sc), false, false);
                const auto y = __builtin_amdgcn_permlane32_swap(cvtpk(a[4 * g + 2] * sc, a[4 * g + 3] * sc), cvtpk(a[4 * g + 6] * sc, a[4 * g + 7] * sc), false, false);
                *(LDSP u32x4*)lds_p(o + 16 * g) = u32x4{x[0], y[0], x[1], y[1]};
            }
        }
    } else if (QKV && kv) {
        int g = 4 * blk + p;
        if (cb == 0) g = g == 1 ? 2 : g == 2 ? 1 : g;
        g = g < 5 ? g : 5;
        const unsigned wkv = G_KV + (8 * h + q) * 48 + g * 8;
        if (cb == 0) gemm_loop<48, false>(acc, xa, xs0, xs1, h, wkv, std::make_integer_sequence<int, 36>{});
        else gemm_loop<48, true>(acc, xa, xs0, xs1, h, wkv, std::make_integer_sequence<int, 36>{});
#pragma unroll
        for (int tt = 0; tt < 2; ++tt) {
            const f32x16& a = acc[tt];
            const int tile = 2 * rb + tt;
            const __amdgpu_buffer_rsrc_t isrd = make_srd(kvimg, 163840 + 256);
            const unsigned img = tile * TILE_B;
            if (cb == 0) {
                const unsigned a0 = cvtpk(a[0], a[1]), a1 = cvtpk(a[2], a[3]), a2 = cvtpk(a[4], a[5]);
                const unsigned a3 = cvtpk(a[8], a[9]), a4 = cvtpk(a[10], a[11]);
                const auto x = __builtin_amdgcn_permlane32_swap(a0, a3, false, false);
                const auto y = __builtin_amdgcn_permlane32_swap(a4, a4, false, false);
                const u32x4 kf = h == 0 ? u32x4{a0, a1, a2, x[1]} : u32x4{a1, a2, x[0], y[0]};
                st128<WB>(isrd, img + lane * 16, kf);
                if (h == 1) st32<WB>(isrd, img + KV_K1 + l31 * 4, a3);
                float n2 = dot2(kf[0], kf[0], dot2(kf[1], kf[1], dot2(kf[2], kf[2], dot2(kf[3], kf[3], h == 1 ? dot2(a3, a3, 0.f) : 0.f))));
                n2 = wave_max(n2 + other_half(n2));
                if (lane == 0) st32<WB>(isrd, 163840 + tile * 4, __float_as_uint(n2));
            } else {
                const int gg = l31 >> 2, e = l31 & 3;
                int dim = -1;
                if (gg < 6) { if (gg & 1) { if (e < 2) dim = 6 * (gg >> 1) + 4 + e; } else dim = 6 * (gg >> 1) + e; }
                int rho = -1;
                if (dim >= 0) rho = dim < 4 ? dim : dim < 14 ? dim + 5 : dim - 10;
                if (l31 == 6) rho = 8;
                if (l31 == 7) rho = 19;
                if (rho >= 0) {
#pragma unroll
                    for (int s = 0; s < 2; ++s) {
                        u32x4 v = pack8(a, s);
                        if (l31 == 6) v = u32x4{ONES, ONES, ONES, ONES};
                        if (l31 == 7) v = u32x4{0u, 0u, 0u, 0u};
                        st128<WB>(isrd, img + KV_V + (2 * s + h) * KV_VH + rho * 16, v);
                    }
                }
            }
        }
    } else {
        if (QKV) {
            constexpr unsigned DUMMY = G_NBUF * G_BUF;
            {
                const int n = ((cb - 2) * 32 + rb) * 5 + lane;
                const char* p = n < 128 ? (const char*)&kTab2 + n * 128 : n < 448 ? (const char*)&kTab3 + (n - 128) * 128
                              : n < 528 ? code_of_k2() + (n - 448) * 128 : code_of_k3() + (n < 590 ? n - 528 : 0) * 128;
                if (lane < 5) glds4(p, DUMMY);
            }
            const int slot = (rb >> 3) * 4 + (cb - 2);
            for (int i = lane; i < 80; i += 64) glds4((const char*)X + (size_t)(t0 + (cb - 2) * 16 + i / 5) * (DM * 2) + 512 + (i % 5) * 128, DUMMY);
            for (int i = lane; i < 180; i += 64) { const int n = slot * 180 + i; glds4((const char*)W + (size_t)(256 + n / 9) * (DM * 2) + (n % 9) * 128, DUMMY); }
        }
#pragma unroll
        for (int c = 0; c < 8; ++c) asm volatile("s_barrier" ::: "memory");
        wait_vm<0>();
    }
    bar();
    const __amdgpu_buffer_rsrc_t osrd = make_srd(Out, SEQ * out_stride * 2);
#pragma unroll
    for (int i = 0; i < 3; ++i) {
        const int n = i * 256 + tid, row = n / 12, c = n % 12;
        st128<QKV ? WB : SC1>(osrd, ((t0 + row) * out_stride + n0) * 2 + c * 16, lds_r128(G_STAGE + row * G_SROW + c * 16));
    }
}

constexpr int F1_X = 0, F1_U = 36864, F1_T = F1_U + 40960, F_TROW = YS * 4, F1_C = F1_T + 37888;
constexpr int FC_G = 0, FC_A = 4096;

DI void fft1_role(int s2, const u16* __restrict__ XB, unsigned* __restrict__ Y) {
    const int tid = threadIdx.x, lane = tid & 63, l31 = lane & 31, h = lane >> 5;
    const int w = rfl(tid >> 6);
    for (int p = w; p < 36; p += 12) {
        const int idx = p * 64 + lane, row = idx / 72, c = idx % 72;
        glds16((const char*)XB + (size_t)(64 * row + s2) * (DM * 2) + c * 16, F1_X + p * 1024);
    }
    const __amdgpu_buffer_rsrc_t ysrd = make_srd(Y, 32 * 64 * YS * 4);
    auto copy_out = [&]() {
        for (int i = tid; i < 32 * 73; i += 768) {
            const int row = i / 73, c = i % 73;
            st128<WB>(ysrd, ((row * 64 + s2) * YS + 4 * c) * 4, lds_r128(F1_T + row * F_TROW + c * 16));
        }
    };
    auto a_table = [&]() {
        for (int i = tid - 512; i < 384; i += 256) {
            const int fl = i / 6, dw = i % 6, r31 = fl & 31, fh = fl >> 5;
            const int reg = (r31 & 3) + 4 * (r31 >> 3), hD = (r31 >> 2) & 1, jj = 5 * hD + (reg >> 1), c = reg & 1;
            const int d1 = dw < 4 ? 8 * fh + 2 * dw : 16 + 4 * fh + 2 * (dw - 4);
            const float q = c ? 0.25f : 0.f;
            const float v0 = cosr((float)(d1 * jj) * (1.0f / 18.0f) + q), v1 = cosr((float)((d1 + 1) * jj) * (1.0f / 18.0f) + q);
            lds_w32(F1_C + FC_A + fl * 32 + dw * 4, cvtpk(reg < 10 && d1 < 18 ? v0 : 0.f, reg < 10 && d1 + 1 < 18 ? v1 : 0.f));
        }
    };
    if (w >= 10) {
        a_table();
        wait_vm<0>();
        bar(); bar(); bar();
        copy_out();
        return;
    }
    const int j1 = w, kd = j1 + 18 * l31;
    const bool mir = kd > 288;
    u32x4 bre[4], bim[4];
#pragma unroll
    for (int t = 0; t < 4; ++t)
        asm volatile("global_load_dwordx4 %0, %1, off" : "=v"(bre[t]) : "v"(kTab1.v[w][t][lane]) : "memory");
    if (w < 8) {
        const int u = tid >> 8, fl = (tid >> 2) & 63, jp = tid & 3;
        const int s1 = 16 * u + 8 * (jp >> 1) + 4 * (fl >> 5) + 2 * (jp & 1), k1 = fl & 31;
        const float r0 = (float)(((64 * s1 + s2) * k1) & 2047) * (1.0f / 2048.0f);
        const float r1 = (float)(((64 * s1 + 64 + s2) * k1) & 2047) * (1.0f / 2048.0f);
        lds_w32(F1_C + FC_G + (2 * u) * 1024 + fl * 16 + jp * 4, cvtpk(cosr(r0), cosr(r1)));
        lds_w32(F1_C + FC_G + (2 * u + 1) * 1024 + fl * 16 + jp * 4, cvtpk(sinr(r0), sinr(r1)));
    } else {
        a_table();
    }
    asm volatile("s_waitcnt vmcnt(0)" : "+v"(bre[0]), "+v"(bre[1]), "+v"(bre[2]), "+v"(bre[3]) :: "memory");
    {
        const unsigned sg = mir ? 0x80000000u : 0x00008000u;
#pragma unroll
        for (int t = 0; t < 4; ++t)
#pragma unroll
            for (int i = 0; i < 4; ++i) bim[t][i] = __builtin_amdgcn_alignbit(bre[t][i], bre[t][i], 16) ^ sg;
    }
    bar();
    if (w < 8) {
        const u32x4 a0 = lds_r128(F1_C + FC_A + lane * 32);
        const u32x2 a1 = lds_r64(F1_C + FC_A + lane * 32 + 16);
        const int blk = (lane >> 4) & 1, q = (lane & 15) >> 2, p = lane & 3;
        const unsigned col = (16 * blk + 4 * p) * 2;
        const unsigned r0 = (8 * h + q) * 64 + col;
        const int d1c = 16 + 4 * h + q;
        const unsigned r1 = (d1c < 17 ? d1c : 17) * 64 + col;
        u32x2 b0[4], b1[4], b2[4];
#pragma unroll
        for (int i = 0; i < 4; ++i) {
            const unsigned base = F1_X + (4 * w + i) * 1152;
            b0[i] = lds_tr(base + r0); b1[i] = lds_tr(base + r0 + 256); b2[i] = lds_tr(base + r1);
        }
        f32x16 acc[4];
#pragma unroll
        for (int i = 0; i < 4; ++i) {
            acc[i] = mfma16(a0, u32x4{b0[i][0], b0[i][1], b1[i][0], b1[i][1]}, f32x16{});
            acc[i] = mfma8(a1, b2[i], acc[i]);
        }
#pragma unroll
        for (int i = 0; i < 4; ++i) {
            const int s1 = 4 * w + i;
            const unsigned ub = F1_U + (5 * h) * 4096 + s1 * 128 + (((l31 >> 2) ^ ((s1 >> 1) & 7)) << 4) + (l31 & 3) * 4;
#pragma unroll
            for (int pp = 0; pp < 5; ++pp) lds_w32(ub + pp * 4096, cvtpk(acc[i][2 * pp], acc[i][2 * pp + 1]));
        }
    }
    bar();
    {
        const unsigned ub = F1_U + j1 * 4096 + l31 * 128;
        const int sw = (l31 >> 1) & 7;
        u32x4 a[4], gc[2], gs[2];
#pragma unroll
        for (int t = 0; t < 4; ++t) a[t] = lds_r128(ub + (((2 * t + h) ^ sw) << 4));
#pragma unroll
        for (int u = 0; u < 2; ++u) { gc[u] = lds_r128(F1_C + FC_G + (2 * u) * 1024 + lane * 16); gs[u] = lds_r128(F1_C + FC_G + (2 * u + 1) * 1024 + lane * 16); }
        f32x16 dre = {}, dim = {};
#pragma unroll
        for (int t = 0; t < 4; ++t) { dre = mfma16(a[t], bre[t], dre); dim = mfma16(a[t], bim[t], dim); }
        f32x16 ore = {}, oim = {};
#pragma unroll
        for (int u = 0; u < 2; ++u) {
            const u32x4 pr = pack8(dre, u), pi = pack8(dim, u);
            ore = mfma16(gc[u], pr, ore);
            oim = mfma16(gc[u], pi, oim);
            ore = mfma16(gs[u], pi, ore);
            oim = mfma16(gs[u], xor4(pr, 0x80008000u), oim);
        }
        if (!(mir && (j1 == 0 || j1 == 9))) {
            const unsigned tb = F1_T + (4 * h) * F_TROW + (mir ? 576 - kd : kd) * 4;
#pragma unroll
            for (int r = 0; r < 16; ++r) lds_w32(tb + crow(r, 0) * F_TROW, cvtpk(ore[r], oim[r]));
        }
    }
    bar();
    copy_out();
}

template <int SOFF> DI void load_bf16(float& d, __amdgpu_buffer_rsrc_t srd, unsigned voff) {
    unsigned so;
    asm volatile("s_mov_b32 %1, %4\n\tbuffer_load_short_d16_hi %0, %2, %3, %1 offen" : "=v"(d), "=&s"(so) : "v"(voff), "s"(srd), "i"(SOFF) : "memory");
}
template <int... Rs>
DI void load_filter(float (&a)[16], float (&b)[16], __amdgpu_buffer_rsrc_t rs, __amdgpu_buffer_rsrc_t is, unsigned voff, std::integer_sequence<int, Rs...>) {
    ((load_bf16<(16 * (Rs >> 2) + (Rs & 3)) * (9248 * 2)>(a[Rs], rs, voff), load_bf16<(16 * (Rs >> 2) + (Rs & 3)) * (9248 * 2)>(b[Rs], is, voff)), ...);
}
#define PIN16(a) asm volatile("" : "+v"(a[0]), "+v"(a[1]), "+v"(a[2]), "+v"(a[3]), "+v"(a[4]), "+v"(a[5]), "+v"(a[6]), "+v"(a[7]), \
                                   "+v"(a[8]), "+v"(a[9]), "+v"(a[10]), "+v"(a[11]), "+v"(a[12]), "+v"(a[13]), "+v"(a[14]), "+v"(a[15]))

DI void fft2_task(int task, const unsigned* Y, unsigned* Z,
                  const u16* __restrict__ WR, const u16* __restrict__ WI) {
    const int lane = threadIdx.x & 63, n = lane & 15, kq = lane >> 4;
    const int cid = 16 * task + n, k1 = cid / 289, kd = cid - 289 * k1;
    const unsigned yoff = ((k1 * 64 + 4 * kq) * YS + kd) * 4;
    u32x4 fre[2], fim[2], cm[4][2], sm[4][2];
    {
        const __amdgpu_buffer_rsrc_t ysrd = make_srd(Y, 32 * 64 * YS * 4);
        unsigned yr[2][8];
#pragma unroll
        for (int ks = 0; ks < 2; ++ks)
#pragma unroll
            for (int j = 0; j < 8; ++j)
                yr[ks][j] = __builtin_amdgcn_raw_buffer_load_b32(ysrd, yoff, (32 * ks + 16 * (j >> 2) + (j & 3)) * (YS * 4), 0);
#pragma unroll
        for (int m = 0; m < 4; ++m)
#pragma unroll
            for (int ks = 0; ks < 2; ++ks) { cm[m][ks] = *(const u32x4*)kTab2.v[m][ks][0][lane]; sm[m][ks] = *(const u32x4*)kTab2.v[m][ks][1][lane]; }
#pragma unroll
        for (int ks = 0; ks < 2; ++ks)
#pragma unroll
            for (int i = 0; i < 4; ++i) {
                fre[ks][i] = __builtin_amdgcn_perm(yr[ks][2 * i + 1], yr[ks][2 * i], 0x05040100u);
                fim[ks][i] = __builtin_amdgcn_perm(yr[ks][2 * i + 1], yr[ks][2 * i], 0x07060302u);
            }
#pragma unroll
        for (int ks = 0; ks < 2; ++ks) { PIN(fre[ks]); PIN(fim[ks]); }
    }
    SB();
    float wa[16], wb[16];
    load_filter(wa, wb, make_srd(WR, 2048 * 289 * 2), make_srd(WI, 2048 * 289 * 2), (cid + 4 * kq * 9248) * 2, std::make_integer_sequence<int, 16>{});
    SB();
    f32x4 xre[4], xim[4];
    {
        const u32x4 nre[2] = {xor4(fre[0], 0x80008000u), xor4(fre[1], 0x80008000u)};
#pragma unroll
        for (int m = 0; m < 4; ++m) {
            xre[m] = mfmas(cm[m][0], fre[0], f32x4{});
            xim[m] = mfmas(cm[m][0], fim[0], f32x4{});
            xre[m] = mfmas(cm[m][1], fre[1], xre[m]);
            xim[m] = mfmas(cm[m][1], fim[1], xim[m]);
            xre[m] = mfmas(sm[m][0], fim[0], xre[m]);
            xim[m] = mfmas(sm[m][0], nre[0], xim[m]);
            xre[m] = mfmas(sm[m][1], fim[1], xre[m]);
            xim[m] = mfmas(sm[m][1], nre[1], xim[m]);
        }
    }
    SB();
    asm volatile("s_waitcnt vmcnt(0)" ::: "memory");
    PIN16(wa); PIN16(wb);
    u32x4 pre[2], pim[2];
#pragma unroll
    for (int ks = 0; ks < 2; ++ks)
#pragma unroll
        for (int b = 0; b < 2; ++b) {
            const int m = 2 * ks + b;
            float pr[4], pi[4];
#pragma unroll
            for (int r = 0; r < 4; ++r) {
                pr[r] = xre[m][r] * wa[4 * m + r] - xim[m][r] * wb[4 * m + r];
                pi[r] = xre[m][r] * wb[4 * m + r] + xim[m][r] * wa[4 * m + r];
            }
            pre[ks][2 * b] = cvtpk(pr[0], pr[1]); pre[ks][2 * b + 1] = cvtpk(pr[2], pr[3]);
            pim[ks][2 * b] = cvtpk(pi[0], pi[1]); pim[ks][2 * b + 1] = cvtpk(pi[2], pi[3]);
        }
    SB();
    const __amdgpu_buffer_rsrc_t zsrd = make_srd(Z, 32 * 64 * YS * 4);
    const u32x4 nim[2] = {xor4(pim[0], 0x80008000u), xor4(pim[1], 0x80008000u)};
#pragma unroll
    for (int m = 0; m < 4; ++m) {
        f32x4 zre = mfmas(cm[m][0], pre[0], f32x4{});
        f32x4 zim = mfmas(cm[m][0], pim[0], f32x4{});
        zre = mfmas(cm[m][1], pre[1], zre);
        zim = mfmas(cm[m][1], pim[1], zim);
        zre = mfmas(sm[m][0], nim[0], zre);
        zim = mfmas(sm[m][0], pre[0], zim);
        zre = mfmas(sm[m][1], nim[1], zre);
        zim = mfmas(sm[m][1], pre[1], zim);
#pragma unroll
        for (int r = 0; r < 4; ++r)
            __builtin_amdgcn_raw_buffer_store_b32(cvtpk(zre[r], zim[r]), zsrd, yoff, (16 * m + r) * (YS * 4), SC1);
    }
}

constexpr int F3_V = 0, F3_O = 40960, F3_OROW = 18 * 80, F3_T = F3_O + 32 * F3_OROW, F3_C = F3_T + 37888;

DI void fft3_role(int s2, const unsigned* __restrict__ Z, u16* __restrict__ out) {
    const int tid = threadIdx.x, lane = tid & 63, l31 = lane & 31, h = lane >> 5;
    const int w = rfl(tid >> 6);
    for (int p = w; p < 37; p += 12) {
        int idx = p * 64 + lane;
        idx = idx < 32 * 73 ? idx : 32 * 73 - 1;
        const int row = idx / 73, c = idx % 73;
        glds16((const char*)(Z + (size_t)(row * 64 + s2) * YS) + c * 16, F3_T + p * 1024);
    }
    u32x4 ec[2], es[2];
#pragma unroll
    for (int u = 0; u < 2; ++u) {
        asm volatile("global_load_dwordx4 %0, %1, off" : "=v"(ec[u]) : "v"(kTab3.v[w < 10 ? w : 0][u][0][lane]) : "memory");
        asm volatile("global_load_dwordx4 %0, %1, off" : "=v"(es[u]) : "v"(kTab3.v[w < 10 ? w : 0][u][1][lane]) : "memory");
    }
    if (w < 8) {
        const int u = tid >> 8, fl = (tid >> 2) & 63, jp = tid & 3;
        const int k1 = 16 * u + 8 * (fl >> 5) + 2 * jp, s = 64 * (fl & 31) + s2;
        const float r0 = (float)((s * k1) & 2047) * (1.0f / 2048.0f), r1 = (float)((s * (k1 + 1)) & 2047) * (1.0f / 2048.0f);
        lds_w32(F3_C + FC_G + (2 * u) * 1024 + fl * 16 + jp * 4, cvtpk(cosr(r0), cosr(r1)));
        lds_w32(F3_C + FC_G + (2 * u + 1) * 1024 + fl * 16 + jp * 4, cvtpk(sinr(r0), sinr(r1)));
    } else {
        for (int i = tid - 512; i < 384; i += 256) {
            const int fl = i / 6, dw = i % 6, d1 = fl & 31, fh = fl >> 5;
            const int jj = dw < 4 ? 4 * fh + dw : 8 + (dw - 4);
            const float r = (float)(d1 * jj) * (1.0f / 18.0f);
            constexpr float sc = 2.0f / (2048.0f * 576.0f);
            float c = sc * cosr(r), sn = -sc * sinr(r);
            if (jj == 0 || jj == 9) { c *= 0.5f; sn = 0.f; }
            if (d1 >= 18 || (dw >= 4 && fh == 1)) { c = 0.f; sn = 0.f; }
            lds_w32(F3_C + FC_A + fl * 32 + dw * 4, cvtpk(c, sn));
        }
    }
    const int j1 = w;
    u32x4 ecs[2], nes[2];
    asm volatile("s_waitcnt vmcnt(0)" : "+v"(ec[0]), "+v"(ec[1]), "+v"(es[0]), "+v"(es[1]) :: "memory");
#pragma unroll
    for (int u = 0; u < 2; ++u)
#pragma unroll
        for (int i = 0; i < 4; ++i) {
            const int kd = j1 + 18 * (16 * u + 8 * (i >> 1) + 4 * h + 2 * (i & 1));
            const unsigned sg = (kd > 288 ? 0x8000u : 0u) | (kd + 18 > 288 ? 0x80000000u : 0u);
            const unsigned keep = (kd == 0 || kd == 288) ? 0xffff0000u : 0xffffffffu;
            ecs[u][i] = (ec[u][i] ^ sg) & keep;
            nes[u][i] = (es[u][i] ^ sg ^ 0x80008000u) & keep;
        }
    bar();
    if (w < 10) {
        const int kd = j1 + 18 * l31;
        const unsigned tb = F3_T + (8 * h) * F_TROW + (kd > 288 ? 576 - kd : kd) * 4;
        unsigned zr[2][8];
        u32x4 gc[2], gs[2];
#pragma unroll
        for (int u = 0; u < 2; ++u)
#pragma unroll
            for (int j = 0; j < 8; ++j) zr[u][j] = *(const LDSP unsigned*)lds_p(tb + (16 * u + j) * F_TROW);
#pragma unroll
        for (int u = 0; u < 2; ++u) { gc[u] = lds_r128(F3_C + FC_G + (2 * u) * 1024 + lane * 16); gs[u] = lds_r128(F3_C + FC_G + (2 * u + 1) * 1024 + lane * 16); }
        f32x16 dre = {}, dim = {};
#pragma unroll
        for (int u = 0; u < 2; ++u) {
            u32x4 fr, fi;
#pragma unroll
            for (int i = 0; i < 4; ++i) {
                fr[i] = __builtin_amdgcn_perm(zr[u][2 * i + 1], zr[u][2 * i], 0x05040100u);
                fi[i] = __builtin_amdgcn_perm(zr[u][2 * i + 1], zr[u][2 * i], 0x07060302u);
            }
            dre = mfma16(fr, gc[u], dre);
            dim = mfma16(fi, gc[u], dim);
            dre = mfma16(xor4(fi, 0x80008000u), gs[u], dre);
            dim = mfma16(fr, gs[u], dim);
        }
        f32x16 tre = {}, tim = {};
#pragma unroll
        for (int u = 0; u < 2; ++u) {
            const u32x4 pr = pack8(dre, u), pi = pack8(dim, u);
            tre = mfma16(pr, ec[u], tre);
            tim = mfma16(pi, ecs[u], tim);
            tre = mfma16(pi, nes[u], tre);
            tim = mfma16(pr, es[u], tim);
        }
        const unsigned vb = F3_V + j1 * 4096 + (4 * h) * 128 + l31 * 4;
#pragma unroll
        for (int r = 0; r < 16; ++r) lds_w32(vb + crow(r, 0) * 128, cvtpk(tre[r], tim[r]));
    }
    bar();
    if (w < 8) {
        const u32x4 b0 = lds_r128(F3_C + FC_A + lane * 32);
        const u32x2 b1 = lds_r64(F3_C + FC_A + lane * 32 + 16);
        u32x4 a0[4]; u32x2 a1[4]; f32x16 acc[4];
#pragma unroll
        for (int i = 0; i < 4; ++i) {
            const unsigned vb = F3_V + (4 * w + i) * 128 + l31 * 4;
#pragma unroll
            for (int e = 0; e < 4; ++e) a0[i][e] = lds_r32(vb + (4 * h + e) * 4096);
            a1[i] = u32x2{lds_r32(vb + 8 * 4096), lds_r32(vb + 9 * 4096)};
        }
#pragma unroll
        for (int i = 0; i < 4; ++i) { acc[i] = mfma16(a0[i], b0, f32x16{}); acc[i] = mfma8(a1[i], b1, acc[i]); }
        if (l31 < 18) {
#pragma unroll
            for (int i = 0; i < 4; ++i)
#pragma unroll
                for (int g = 0; g < 4; ++g)
                    *(LDSP u32x2*)lds_p(F3_O + (4 * w + i) * F3_OROW + l31 * 80 + (8 * g + 4 * h) * 2) =
                        u32x2{cvtpk(acc[i][4 * g], acc[i][4 * g + 1]), cvtpk(acc[i][4 * g + 2], acc[i][4 * g + 3])};
        }
        asm volatile("s_waitcnt lgkmcnt(0)" ::: "memory");
        const __amdgpu_buffer_rsrc_t osrd = make_srd(out, SEQ * 2 * DM * 2);
        for (int i = lane; i < 288; i += 64) {
            const int rr = i / 72, c = i % 72, s1 = 4 * w + rr;
            st128(osrd, (64 * s1 + s2) * 2304 + 1152 + c * 16, lds_r128(F3_O + s1 * F3_OROW + (c >> 2) * 80 + (c & 3) * 16));
        }
    }
}

constexpr int NQ = 4;
struct Attn {
    u32x4 q0[NQ]; u32x2 q1[NQ]; f32x16 o[NQ];
    f32x16 s[2]; u32x4 p0, p1, kf, v0, v1; u32x2 kg;
    unsigned ka, kb, va;
};
#define EX(s, i) s[i] = __builtin_amdgcn_exp2f(s[i])

template <int I> DI void attn_step(Attn& st, unsigned ba) {
    constexpr int tp = I / NQ, q = I % NQ, cur = I & 1, nxt = cur ^ 1;
    constexpr int qn = (I + 1) % NQ, qp = (I + NQ - 1) % NQ;
    const f32x16 zero = {};
    f32x16& C = st.s[cur]; f32x16& N = st.s[nxt];
    const unsigned bn = ba + (tp + 1) * TILE_B;
    N = mfma16(st.kf, st.q0[qn], zero); SB();
    EX(C, 0); EX(C, 1); EX(C, 2); PIN(C); SB();
    st.o[qp] = mfma16(st.v1, st.p1, st.o[qp]); SB();
    if constexpr (q == NQ - 2) st.kf = lds_r128(bn + st.ka);
    if constexpr (q == 0) st.v1 = lds_r128(ba + tp * TILE_B + st.va + 2 * KV_VH);
    EX(C, 3); EX(C, 4); EX(C, 5); PIN(C); SB();
    N = mfma8(st.kg, st.q1[qn], N); SB();
    if constexpr (q == NQ - 2) st.kg[0] = lds_r32(bn + st.kb);
    EX(C, 6); EX(C, 7);
    st.p0[0] = cvtpk(C[0], C[1]); st.p0[1] = cvtpk(C[2], C[3]); st.p0[2] = cvtpk(C[4], C[5]);
    EX(C, 8); st.p0[3] = cvtpk(C[6], C[7]); EX(C, 9);
    PIN(st.p0); PIN(C); SB();
    st.o[q] = mfma16(st.v0, st.p0, st.o[q]); SB();
    if constexpr (q == NQ - 1) st.v0 = lds_r128(bn + st.va);
    EX(C, 10); EX(C, 11); EX(C, 12); EX(C, 13); EX(C, 14); EX(C, 15);
    st.p1[0] = cvtpk(C[8], C[9]); st.p1[1] = cvtpk(C[10], C[11]); st.p1[2] = cvtpk(C[12], C[13]); st.p1[3] = cvtpk(C[14], C[15]);
    PIN(st.p1); SB();
}
template <int I0, int... Is> DI void attn_steps(Attn& st, unsigned ba, std::integer_sequence<int, Is...>) { (attn_step<I0 + Is>(st, ba), ...); }
DI unsigned attn_pair(Attn& st, int t, unsigned word, bool first) {
    attn_steps<0>(st, t * TILE_B, std::make_integer_sequence<int, NQ - 1>{});
    if (first)
        while (lds_poll(word) < 1) __builtin_amdgcn_s_sleep(1);
    attn_steps<NQ - 1>(st, t * TILE_B, std::make_integer_sequence<int, NQ - 1>{});
    const unsigned v = *(const volatile LDSP unsigned*)lds_p(word);
    SB();
    attn_steps<2 * NQ - 2>(st, t * TILE_B, std::make_integer_sequence<int, 2>{});
    return v;
}
DI void attn_begin(Attn& st, int t0) {
    st.kf = lds_r128(t0 * TILE_B + st.ka); st.kg = u32x2{lds_r32(t0 * TILE_B + st.kb), (threadIdx.x & 32) ? 0u : ONES};
    st.v0 = lds_r128(t0 * TILE_B + st.va);
    st.s[0] = mfma16(st.kf, st.q0[0], f32x16{}); st.s[0] = mfma8(st.kg, st.q1[0], st.s[0]);
    st.p1 = u32x4{0, 0, 0, 0}; st.v1 = u32x4{0, 0, 0, 0};
}
DI void attn_end(Attn& st) { st.o[NQ - 1] = mfma16(st.v1, st.p1, st.o[NQ - 1]); }

DI u32x2 bias_frag(float m, unsigned q16, int h) {
    const unsigned hi = cvtpk(-m, 0.f) & 0xffffu;
    const unsigned lo = cvtpk(-m - bf_lo(hi), 0.f) & 0xffffu;
    return h == 0 ? u32x2{q16, hi | (lo << 16)} : u32x2{0u, 0u};
}

DI void attn_exact(int token, const u16* Q, const char* __restrict__ kvimg, unsigned ka, unsigned kb, unsigned va,
                   float (&o)[11]) {
    int lane = threadIdx.x & 63;
    PIN(lane);
    const int l31 = lane & 31, h = lane >> 5;
    const u16* qp = Q + (size_t)token * DM + l31 * DH;
    const unsigned* q32 = (const unsigned*)(qp + 8 * h);
    const u32x4 q0 = {q32[0], q32[1], q32[2], q32[3]};
    const unsigned q16 = *(const unsigned*)(qp + 16);
    u32x2 q1 = bias_frag(0.f, q16, h);
    float mx = -3.0e38f;
#pragma unroll 1
    for (int t = 0; t < 64; ++t) {
        const char* b = kvimg + t * TILE_B;
        f32x16 s = mfma16(*(const u32x4*)(b + ka), q0, f32x16{});
        s = mfma8(u32x2{*(const unsigned*)(b + kb), ONES}, q1, s);
#pragma unroll
        for (int r = 0; r < 16; ++r) mx = fmaxf(mx, s[r]);
    }
    q1 = bias_frag(fmaxf(mx, other_half(mx)), q16, h);
    f32x16 acc = {};
#pragma unroll 1
    for (int t = 0; t < 64; ++t) {
        const char* b = kvimg + t * TILE_B;
        f32x16 s = mfma16(*(const u32x4*)(b + ka), q0, f32x16{});
        s = mfma8(u32x2{*(const unsigned*)(b + kb), ONES}, q1, s);
#pragma unroll
        for (int r = 0; r < 16; ++r) s[r] = __builtin_amdgcn_exp2f(s[r]);
        acc = mfma16(*(const u32x4*)(b + va), pack8(s, 0), acc);
        acc = mfma16(*(const u32x4*)(b + va + 2 * KV_VH), pack8(s, 1), acc);
    }
#pragma unroll
    for (int r = 0; r < 11; ++r) o[r] = acc[r];
}

constexpr int A_LANDED = 64 * TILE_B, A_PASSED = A_LANDED + 64, A_DUMMY = A_LANDED + 256;
constexpr int A_QUART = 16 * TILE_B, A_PARTW = 9 * 1024, A_STAGE = 8 * TILE_B;
constexpr int ATTN_LEAD = 6;

DI void attn_role(int wg, const u16* Q, const char* __restrict__ kvimg, const float* __restrict__ kmax,
                  u16* O, const unsigned* Y, unsigned* Z,
                  const u16* __restrict__ WR, const u16* __restrict__ WI, const u16* __restrict__ WO) {
    const int tid = threadIdx.x, lane = tid & 63, l31 = lane & 31, h = lane >> 5;
    const int w = rfl(tid >> 6);
    if (w >= 8) {
        const int g = w - 8;
        int task = -1;
        const int slot = (g - wg) & 3;
        if (slot < 2) task = 2 * wg + slot;
        else if (slot == 2 && wg < 66) task = 512 + wg;
        __builtin_amdgcn_s_setprio(3);
        if (g == 0 && lane < 32) lds_w32(A_LANDED + lane * 4, 0);
        const unsigned dst = g * A_QUART;
        const char* src = kvimg + dst + lane * 16;
#pragma unroll
        for (int j = 4; j < 8; ++j) glds16(src + j * 1024, dst + j * 1024);
        bar();
        wait_vm<0>();
        lds_post(A_LANDED + g * 4, 1);
#pragma unroll
        for (int j = 8; j < 38; ++j) glds16(src + j * 1024, dst + j * 1024);
        wait_vm<16>();
        lds_post(A_LANDED + g * 4, 2);
        wait_vm<0>();
        lds_post(A_LANDED + g * 4, 3);
        if (lane < 6) glds4((const char*)WO + ((wg * 4 + g) * 6 + lane < 5184 ? ((wg * 4 + g) * 6 + lane) * 128 : 0), A_DUMMY);
        __builtin_amdgcn_s_setprio(1);
        if (task >= 0) fft2_task(task, Y, Z, WR, WI);
        __builtin_amdgcn_s_setprio(0);
        wait_vm<0>();
        bar();
        return;
    }
    const int g = w & 3, rg = w >> 2;
    const int tb = 16 * g;
    {
        const unsigned kdst = g * A_QUART + rg * 1024;
        const char* ksrc = kvimg + kdst + lane * 16;
#pragma unroll
        for (int j = 0; j < 2; ++j) glds16(ksrc + j * 2048, kdst + j * 2048);
    }
    Attn st;
    {
        unsigned q16[NQ];
        float km = kmax[lane];
        const u16* qp = Q + (size_t)(wg * 8 + 4 * rg) * DM + l31 * DH;
#pragma unroll
        for (int q = 0; q < NQ; ++q) {
            const unsigned* q32 = (const unsigned*)(qp + q * DM + 8 * h);
            st.q0[q] = u32x4{q32[0], q32[1], q32[2], q32[3]};
            q16[q] = *(const unsigned*)(qp + q * DM + 16);
        }
        km = wave_max(km);
#pragma unroll
        for (int q = 0; q < NQ; ++q) {
            float n2 = h == 0 ? dot2(q16[q], q16[q], 0.f) : 0.f;
#pragma unroll
            for (int i = 0; i < 4; ++i) n2 = dot2(st.q0[q][i], st.q0[q][i], n2);
            n2 += other_half(n2);
            st.q1[q] = bias_frag(__builtin_amdgcn_sqrtf(n2 * km), q16[q], h);
        }
    }
    st.ka = lane * 16;
    st.kb = h == 0 ? KV_K1 + l31 * 4 : KV_V + 19 * 16;
    {
        const int gg = l31 >> 2, e = l31 & 3;
        int rho = 19;
        if (gg == 0) rho = e;
        else if (gg == 1) rho = 15 + e;
        else if (gg == 2) rho = 9 + e;
        else if (gg == 3) rho = 4 + e;
        else if (gg == 4 && e < 2) rho = 13 + e;
        else if (l31 == 18 || l31 == 20) rho = 8;
        st.va = KV_V + h * KV_VH + rho * 16;
    }
#pragma unroll
    for (int q = 0; q < NQ - 1; ++q) { st.o[q] = f32x16{}; PIN(st.o[q]); }
    wait_vm<0>();
    bar();
    if (rg == 0) __builtin_amdgcn_s_setprio(2); else __builtin_amdgcn_s_setprio(1);
    attn_begin(st, tb);
    SB();
    st.o[NQ - 1] = f32x16{}; PIN(st.o[NQ - 1]);
    SB();
    unsigned landed = 0;
#pragma unroll 1
    for (int t = 0; t < 16; t += 2) {
        if (t == 2 || t == 8) {
            const unsigned need = t == 2 ? 2 : 3;
            if (rfl(landed) < need)
                while (lds_poll(A_LANDED + g * 4) < need) __builtin_amdgcn_s_sleep(1);
        }
        landed = attn_pair(st, tb + t, A_LANDED + g * 4, t == 0);
        if (t == 6) lds_post(A_PASSED + w * 4, 1);
        if (t == 2 * ATTN_LEAD - 2 && rg == 0) __builtin_amdgcn_s_setprio(0);
    }
    attn_end(st);

    int ln = threadIdx.x & 63;
    PIN(ln);
    while (lds_poll(A_PASSED + (w ^ 4) * 4) == 0) __builtin_amdgcn_s_sleep(1);
    float o[11];
#pragma unroll
    for (int q = 0; q < NQ; ++q) {
        if (q == g) {
#pragma unroll
            for (int r = 0; r < 11; ++r) o[r] = st.o[q][r];
        } else {
            const unsigned a = g * A_QUART + rg * A_PARTW + (q < g ? q : q - 1) * 3072 + ln * 16;
#pragma unroll
            for (int i = 0; i < 3; ++i)
                *(LDSP f32x4*)lds_p(a + i * 1024) = f32x4{st.o[q][4 * i], st.o[q][4 * i + 1], st.o[q][4 * i + 2], st.o[q][4 * i + 3]};
        }
    }
    __builtin_amdgcn_s_setprio(0);
    bar();
#pragma unroll
    for (int gg = 0; gg < 4; ++gg) {
        if (gg != g) {
            const unsigned a = gg * A_QUART + rg * A_PARTW + (g < gg ? g : g - 1) * 3072 + ln * 16;
#pragma unroll
            for (int i = 0; i < 3; ++i) {
                const f32x4 v = *(const LDSP f32x4*)lds_p(a + i * 1024);
#pragma unroll
                for (int e = 0; e < 4; ++e) if (4 * i + e < 11) o[4 * i + e] += v[e];
            }
        }
    }
    const int token = wg * 8 + 4 * rg + g;
    float l = ln < 32 ? o[10] : o[8];
    if (__any(!(l >= 0x1p-30f && l <= 0x1p+30f))) {
        attn_exact(token, Q, kvimg, st.ka, st.kb, st.va, o);
        l = ln < 32 ? o[10] : o[8];
    }
    const float inv = __builtin_amdgcn_rcpf(l);
    const unsigned stage = g * A_QUART + A_STAGE + rg * 1152;
    const unsigned sa = stage + (ln & 31) * (DH * 2) + ((ln >> 5) ? 20 : 0);
    lds_w32(sa, cvtpk(o[0] * inv, o[1] * inv));
    lds_w32(sa + 4, cvtpk(o[2] * inv, o[3] * inv));
    lds_w32(sa + 8, cvtpk(o[4] * inv, o[5] * inv));
    lds_w32(sa + 12, cvtpk(o[6] * inv, o[7] * inv));
    if (ln < 32) lds_w32(sa + 16, cvtpk(o[8] * inv, o[9] * inv));
    asm volatile("s_waitcnt lgkmcnt(0)" ::: "memory");
    const __amdgpu_buffer_rsrc_t osrd = make_srd(O, SEQ * DM * 2);
    st128(osrd, token * (DM * 2) + ln * 16, lds_r128(stage + ln * 16));
    if (ln < 8) st128(osrd, token * (DM * 2) + 1024 + ln * 16, lds_r128(stage + 1024 + ln * 16));
}

}

__global__ void __launch_bounds__(768, 3) k1_proj_fft1(const u16* ax, const u16* wq, const u16* wk, const u16* wv, const u16* bx, char* ws) {
    const int b = blockIdx.x;
    if (b < 64) {
        fft1_role(b, bx, (unsigned*)(ws + WS_Y));
    } else {
        const int g = b - 64, cb = g / 32, rb = g % 32;
        gemm_role<true>(rb, cb, ax, wq, cb == 0 ? wk : wv, (u16*)(ws + WS_Q), DM, ws + WS_KV);
    }
}

__global__ void __launch_bounds__(768, 3) k2_attn_fft2(const u16* wr, const u16* wi, const u16* wo, char* ws) {
    attn_role(blockIdx.x, (const u16*)(ws + WS_Q), ws + WS_KV, (const float*)(ws + WS_KMAX), (u16*)(ws + WS_O),
              (const unsigned*)(ws + WS_Y), (unsigned*)(ws + WS_Z), wr, wi, wo);
}

__global__ void __launch_bounds__(768, 3) k3_proj_fft3(const u16* wo, char* ws, u16* out) {
    const int b = blockIdx.x;
    if (b < 64) {
        fft3_role(b, (const unsigned*)(ws + WS_Z), out);
    } else {
        const int g = b - 64, cb = g / 32, rb = g % 32;
        gemm_role<false>(rb, cb, (const u16*)(ws + WS_O), wo, wo, out, 2 * DM, ws);
    }
}

extern "C" void kernel_launch(void* const* d_in, const int* in_sizes, int n_in, void* d_out, int out_size, void* d_ws,
                              size_t ws_size, hipStream_t stream) {
    const u16* ax = (const u16*)d_in[0];
    const u16* wq = (const u16*)d_in[1];
    const u16* wk = (const u16*)d_in[2];
    const u16* wv = (const u16*)d_in[3];
    const u16* wo = (const u16*)d_in[4];
    const u16* bx = (const u16*)d_in[5];
    const u16* wr = (const u16*)d_in[6];
    const u16* wi = (const u16*)d_in[7];
    char* ws = (char*)d_ws;
    constexpr int LDS1 = G_NBUF * G_BUF + 256, LDS2 = 163840, LDS3 = G_NBUF * G_BUF;
    (void)hipFuncSetAttribute((const void*)k1_proj_fft1, hipFuncAttributeMaxDynamicSharedMemorySize, LDS1);
    (void)hipFuncSetAttribute((const void*)k2_attn_fft2, hipFuncAttributeMaxDynamicSharedMemorySize, LDS2);
    (void)hipFuncSetAttribute((const void*)k3_proj_fft3, hipFuncAttributeMaxDynamicSharedMemorySize, LDS3);
    k1_proj_fft1<<<256, 768, LDS1, stream>>>(ax, wq, wk, wv, bx, ws);
    k2_attn_fft2<<<256, 768, LDS2, stream>>>(wr, wi, wo, ws);
    k3_proj_fft3<<<256, 768, LDS3, stream>>>(wo, ws, (u16*)d_out);
}
```
